```python
import torch

def _softmax(x, axis=-1):
    return torch.softmax(x, dim=axis)
def _gelu(x):
    return torch.nn.functional.gelu(x, approximate="tanh")
def _silu(x):
    return torch.nn.functional.silu(x)
def _sigmoid(x):
    return torch.sigmoid(x)
def _layernorm(x, axis=-1, eps=1e-5):
    m = x.mean(dim=axis, keepdim=True)
    v = x.var(dim=axis, keepdim=True, unbiased=False)
    return (x - m) / torch.sqrt(v + eps)
def _scatter_add(tgt, idx, src, axis=0):
    return tgt.clone().index_add_(axis, idx, src)
def _segment_sum(src, seg, n):
    out = torch.zeros((n,) + src.shape[1:], dtype=src.dtype)
    return out.index_add_(0, seg, src)
def _pad(x, pw):
    flat = tuple(v for pair in reversed(pw) for v in pair)
    return torch.nn.functional.pad(x, flat)

kernel_name = 'standalone__gqa_softmax__sampling'

def setup_inputs():

    _w = (lambda a: (lambda t: t.to(torch.bfloat16) if t.is_floating_point() else t)(torch.as_tensor(a)))
    B, Hq, Hkv, Sq, Sk, D = 16, 32, 8, 4, 2048, 128
    return {
        "q": _w((lambda *s: torch.randn(*s, dtype=torch.bfloat16) * 0.8)(B, Hq, Sq, D)),
        "k": _w((lambda *s: torch.randn(*s, dtype=torch.bfloat16) * 0.8)(B, Hkv, Sk, D)),
        "v": _w((lambda *s: torch.randn(*s, dtype=torch.bfloat16) * 0.05)(B, Hkv, Sk, D)),
    }

def reference(q, k, v):
    B, Hq, Sq, D = q.shape
    Hkv = k.shape[1]
    rep = Hq // Hkv
    k = (lambda x, n, axis: torch.repeat_interleave(x, n, dim=axis))(k, rep, axis=1)
    v = (lambda x, n, axis: torch.repeat_interleave(x, n, dim=axis))(v, rep, axis=1)
    scale = D ** -0.5
    scores = torch.einsum("bhqd,bhkd->bhqk", q, k) * scale
    p = _softmax(scores, axis=-1)
    return torch.einsum("bhqk,bhkd->bhqd", p, v)
```

```cpp
#include <hip/hip_runtime.h>
#include <hip/hip_bf16.h>
#include <stdint.h>

typedef unsigned u32x4 __attribute__((ext_vector_type(4)));
typedef unsigned u32x2 __attribute__((ext_vector_type(2)));
typedef float f32x4 __attribute__((ext_vector_type(4)));
typedef __bf16 bf16x8 __attribute__((ext_vector_type(8)));
typedef __bf16 bf16x2 __attribute__((ext_vector_type(2)));
typedef float f32x2 __attribute__((ext_vector_type(2)));

#define KV_AUX 2
#define NINFL 32

constexpr int kHeads = 128;
constexpr int kD = 128;
constexpr int kRows = 16;
constexpr unsigned kSlab = 2048u * 256u;
constexpr int kWaves = 8;
constexpr int kLoads = 32;
constexpr int kRowStride = kD + 4;
constexpr float kScaleLog2e = 0.08838834764831845f * 1.4426950408889634f;
constexpr int kRsrcWord3 = 0x00020000;

__device__ __forceinline__ f32x4 mfma16(u32x4 a, u32x4 b, f32x4 c) {
  return __builtin_amdgcn_mfma_f32_16x16x32_bf16(__builtin_bit_cast(bf16x8, a), __builtin_bit_cast(bf16x8, b), c, 0, 0, 0);
}
__device__ __forceinline__ unsigned pack_bf16(float lo, float hi) {
  const bf16x2 r = __builtin_convertvector((f32x2){lo, hi}, bf16x2);
  return __builtin_bit_cast(unsigned, r);
}
__device__ __forceinline__ float xor16_max(float x) { return fmaxf(x, __shfl_xor(x, 16)); }
__device__ __forceinline__ float xor32_max(float x) { return fmaxf(x, __shfl_xor(x, 32)); }

__global__ __launch_bounds__(kWaves * 64, 2) void gqa_partial(const char* __restrict__ Q, const char* __restrict__ K,
                                                               const char* __restrict__ V, float* __restrict__ ws) {
  __shared__ __attribute__((aligned(16))) float sO[kWaves * kRows * kRowStride];
  __shared__ __attribute__((aligned(16))) float sM[kRows * kWaves];
  __shared__ __attribute__((aligned(16))) float sL[kRows * kWaves];

  const int tid = threadIdx.x, lane = tid & 63;
  const int w = __builtin_amdgcn_readfirstlane(tid >> 6);
  const unsigned wg = blockIdx.x;
  const unsigned split = (wg >> 3) & 1;
  const unsigned head = ((wg >> 4) & 7) | ((wg & 7) << 3) | ((wg >> 7) << 6);

  const __amdgpu_buffer_rsrc_t rq = __builtin_amdgcn_make_buffer_rsrc((void*)(Q + (size_t)head * (kRows * kD * 2)), 0, kRows * kD * 2, kRsrcWord3);
  const __amdgpu_buffer_rsrc_t rk = __builtin_amdgcn_make_buffer_rsrc((void*)(K + (size_t)head * kSlab), 0, kSlab, kRsrcWord3);
  const __amdgpu_buffer_rsrc_t rv = __builtin_amdgcn_make_buffer_rsrc((void*)(V + (size_t)head * kSlab), 0, kSlab, kRsrcWord3);

  const int g = lane >> 4, col = lane & 15;
  const unsigned base = (((unsigned)w & 3u) | (((unsigned)w >> 2) << 7) | (split << 8)) << 10;
  const int koff = ((lane >> 2) & 3) * 256 + g * 64 + (lane & 3) * 16;
  const int voff = lane * 16;

  u32x4 qf[4];
#pragma unroll
  for (int kk = 0; kk < 4; ++kk) {
    qf[kk] = __builtin_amdgcn_raw_buffer_load_b128(rq, col * 256 + g * 64 + kk * 16, 0, 0);
    __builtin_amdgcn_sched_barrier(0);
  }

  u32x4 buf[2 * kLoads];
  auto issue = [&](int t) {
    if (t < kLoads) buf[t] = __builtin_amdgcn_raw_buffer_load_b128(rk, koff, base + t * 4096, KV_AUX);
    else buf[t] = __builtin_amdgcn_raw_buffer_load_b128(rv, voff, base + (t - kLoads) * 4096, KV_AUX);
    __builtin_amdgcn_sched_barrier(0);
  };
#pragma unroll
  for (int t = 0; t < NINFL; ++t) issue(t);

  float s[kLoads];
  const f32x4 zero = {0.f, 0.f, 0.f, 0.f};
#pragma unroll
  for (int t0 = 0; t0 < kLoads; t0 += 4) {
#pragma unroll
    for (int t = t0; t < t0 + 4; ++t) {
      const f32x4 d0 = mfma16(buf[t], qf[0], zero);
      const f32x4 d1 = mfma16(buf[t], qf[1], zero);
      const f32x4 d2 = mfma16(buf[t], qf[2], zero);
      const f32x4 d3 = mfma16(buf[t], qf[3], zero);
      s[t] = (d0[0] + d1[1]) + (d2[2] + d3[3]);
    }
    __builtin_amdgcn_sched_barrier(0);
#pragma unroll
    for (int t = t0; t < t0 + 4; ++t)
      if (t + NINFL < 2 * kLoads) issue(t + NINFL);
  }

  float m = s[0];
#pragma unroll
  for (int t = 1; t < kLoads; ++t) m = fmaxf(m, s[t]);
  m = xor32_max(xor16_max(m));
  const float mc = m * kScaleLog2e;
  float l = 0.f;
  unsigned pk[kLoads / 2];
#pragma unroll
  for (int t = 0; t < kLoads; t += 2) {
    const float p0 = __builtin_amdgcn_exp2f(fmaf(s[t], kScaleLog2e, -mc));
    const float p1 = __builtin_amdgcn_exp2f(fmaf(s[t + 1], kScaleLog2e, -mc));
    l += p0 + p1;
    pk[t / 2] = pack_bf16(p0, p1);
  }
  l += __shfl_xor(l, 16);
  l += __shfl_xor(l, 32);
  if (g == 0) {
    sM[col * kWaves + w] = mc;
    sL[col * kWaves + w] = l;
  }
  __builtin_amdgcn_sched_barrier(0);

  f32x4 acc[8];
#pragma unroll
  for (int dd = 0; dd < 8; ++dd) acc[dd] = zero;
#pragma unroll
  for (int u = 0; u < kLoads / 8; ++u) {
    const u32x4 pb = {pk[4 * u], pk[4 * u + 1], pk[4 * u + 2], pk[4 * u + 3]};
    const u32x4* vv = &buf[kLoads + 8 * u];
#pragma unroll
    for (int dd = 0; dd < 8; ++dd) {
      const unsigned sel = (dd & 1) ? 0x07060302u : 0x05040100u;
      u32x4 a;
#pragma unroll
      for (int j = 0; j < 4; ++j) a[j] = __builtin_amdgcn_perm(vv[2 * j + 1][dd >> 1], vv[2 * j][dd >> 1], sel);
      acc[dd] = mfma16(a, pb, acc[dd]);
    }
    __builtin_amdgcn_sched_barrier(0);
#pragma unroll
    for (int t = kLoads + 8 * u; t < kLoads + 8 * u + 8; ++t)
      if (t + NINFL < 2 * kLoads) issue(t + NINFL);
  }

  float* myO = sO + (w * kRows + col) * kRowStride + 32 * g;
#pragma unroll
  for (int r = 0; r < 4; ++r) {
    *(f32x4*)(myO + 8 * r) = (f32x4){acc[0][r], acc[1][r], acc[2][r], acc[3][r]};
    *(f32x4*)(myO + 8 * r + 4) = (f32x4){acc[4][r], acc[5][r], acc[6][r], acc[7][r]};
  }
  __syncthreads();

  const int qr = tid >> 5, d4 = tid & 31;
  const f32x4 m0 = *(const f32x4*)(sM + qr * kWaves), m1 = *(const f32x4*)(sM + qr * kWaves + 4);
  const f32x4 l0 = *(const f32x4*)(sL + qr * kWaves), l1 = *(const f32x4*)(sL + qr * kWaves + 4);
  float mw[8] = {m0[0], m0[1], m0[2], m0[3], m1[0], m1[1], m1[2], m1[3]};
  float lw[8] = {l0[0], l0[1], l0[2], l0[3], l1[0], l1[1], l1[2], l1[3]};
  float M = mw[0];
#pragma unroll
  for (int i = 1; i < 8; ++i) M = fmaxf(M, mw[i]);
  float L = 0.f;
  f32x4 o = zero;
#pragma unroll
  for (int i = 0; i < 8; ++i) {
    const float f = __builtin_amdgcn_exp2f(mw[i] - M);
    L = fmaf(lw[i], f, L);
    const f32x4 x = *(const f32x4*)(sO + (i * kRows + qr) * kRowStride + 4 * d4);
    o += x * f;
  }
  float* part = ws + (size_t)(head * 2 + split) * (kRows * kD + 2 * kRows);
  *(f32x4*)(part + qr * kD + 4 * d4) = o;
  if (d4 == 0) {
    part[kRows * kD + qr] = M;
    part[kRows * kD + kRows + qr] = L;
  }
}

__global__ __launch_bounds__(512) void gqa_combine(const float* __restrict__ ws, u32x2* __restrict__ out) {
  const int tid = threadIdx.x, head = blockIdx.x;
  const int qr = tid >> 5, d4 = tid & 31;
  const float* pa = ws + (size_t)(head * 2) * (kRows * kD + 2 * kRows);
  const float* pb = pa + (kRows * kD + 2 * kRows);
  const f32x4 a = *(const f32x4*)(pa + qr * kD + 4 * d4), b = *(const f32x4*)(pb + qr * kD + 4 * d4);
  const float ma = pa[kRows * kD + qr], mb = pb[kRows * kD + qr];
  const float la = pa[kRows * kD + kRows + qr], lb = pb[kRows * kD + kRows + qr];
  const float M = fmaxf(ma, mb);
  const float fa = __builtin_amdgcn_exp2f(ma - M), fb = __builtin_amdgcn_exp2f(mb - M);
  const float inv = 1.0f / (la * fa + lb * fb);
  const f32x4 o = (a * fa + b * fb) * inv;
  out[(size_t)head * (kRows * kD / 4) + tid] = (u32x2){pack_bf16(o[0], o[1]), pack_bf16(o[2], o[3])};
}

extern "C" void kernel_launch(void* const* d_in, const int* in_sizes, int n_in, void* d_out, int out_size, void* d_ws,
                              size_t ws_size, hipStream_t stream) {
  gqa_partial<<<2 * kHeads, kWaves * 64, 0, stream>>>((const char*)d_in[0], (const char*)d_in[1], (const char*)d_in[2], (float*)d_ws);
  gqa_combine<<<kHeads, 512, 0, stream>>>((const float*)d_ws, (u32x2*)d_out);
}
```

```cpp
#include <hip/hip_runtime.h>
#include <hip/hip_bf16.h>
#include <stdint.h>

typedef unsigned u32x4 __attribute__((ext_vector_type(4)));
typedef unsigned u32x2 __attribute__((ext_vector_type(2)));
typedef float f32x4 __attribute__((ext_vector_type(4)));
typedef float f32x2 __attribute__((ext_vector_type(2)));
typedef short s16x4 __attribute__((ext_vector_type(4)));
typedef __bf16 bf16x8 __attribute__((ext_vector_type(8)));
typedef __bf16 bf16x2 __attribute__((ext_vector_type(2)));
typedef unsigned long long u64;
#define GAS __attribute__((address_space(1)))
typedef GAS unsigned gu32;
typedef GAS u64 gu64;
#define RLX_AGENT __ATOMIC_RELAXED, __HIP_MEMORY_SCOPE_AGENT

constexpr int kInFlight = 32;
constexpr int kExtra = 4;
constexpr int kSweepAt = 4;
constexpr int kPollSleep = 8;

constexpr int kHeads = 128;
constexpr int kD = 128;
constexpr int kRows = 16;
constexpr unsigned kSlab = 2048u * 256u;
constexpr int kWaves = 8;
constexpr int kLoadsA = 32 + kExtra;
constexpr int kLoadsB = 32 - kExtra;
constexpr int kRowStride = kD + 4;
constexpr float kScaleLog2e = 0.08838834764831845f * 1.4426950408889634f;
constexpr int kRsrcWord3 = 0x00020000;
constexpr int kAuxNt = 2;
constexpr int kAuxSc1 = 16;

constexpr unsigned kGranPerHead = 2048 + 32;
constexpr unsigned kGranStride = 17 * 1024;
constexpr unsigned kNonceBase = kHeads * kGranStride;
constexpr unsigned kNonceStride = 256;
constexpr unsigned kTagRange = 0x10000;
constexpr unsigned kSpinCap = 0xF000;

__device__ __forceinline__ f32x4 mfma32(u32x4 a, u32x4 b, f32x4 c) {
  return __builtin_amdgcn_mfma_f32_16x16x32_bf16(__builtin_bit_cast(bf16x8, a), __builtin_bit_cast(bf16x8, b), c, 0, 0, 0);
}
__device__ __forceinline__ f32x4 mfma16(u32x2 a, u32x2 b, f32x4 c) {
  return __builtin_amdgcn_mfma_f32_16x16x16bf16_1k(__builtin_bit_cast(s16x4, a), __builtin_bit_cast(s16x4, b), c, 0, 0, 0);
}
__device__ __forceinline__ unsigned pack_bf16(float lo, float hi) {
  return __builtin_bit_cast(unsigned, __builtin_convertvector((f32x2){lo, hi}, bf16x2));
}
__device__ __forceinline__ u64 granule(float v, unsigned tag) {
  return ((u64)tag << 32) | __builtin_bit_cast(unsigned, v);
}

struct Smem {
  float o[kWaves * kRows * kRowStride];
  float m[kRows * kWaves];
  float l[kRows * kWaves];
};

template <int NL, bool CONSUMER>
__device__ __forceinline__ void attend(const char* __restrict__ Q, const char* __restrict__ K, const char* __restrict__ V,
                                       char* __restrict__ ws, u32x2* __restrict__ out, unsigned head, Smem& sm) {
  constexpr int kSteps = (NL + 7) / 8;

  const int tid = threadIdx.x, lane = tid & 63;
  const int w = __builtin_amdgcn_readfirstlane(tid >> 6);
  const int g = lane >> 4, col = lane & 15;

  gu64* gran = (gu64*)(ws + (size_t)head * kGranStride);
  gu32* nonce_word = (gu32*)(ws + kNonceBase + head * kNonceStride) + w;
  const int qr = tid >> 5, d4 = tid & 31;
  gu64* my_gran = gran + tid;
  gu64* my_stat = gran + 2048 + qr * 2 + (d4 & 1);

  u32x4 old01, old23;
  u32x2 oldst;
  unsigned r0;
  if constexpr (CONSUMER) {
    const __amdgpu_buffer_rsrc_t rg = __builtin_amdgcn_make_buffer_rsrc((void*)gran, 0, kGranPerHead * 8, kRsrcWord3);
    const int off = (g >> 1) * 4096 + w * 512 + (lane & 31) * 16;
    old01 = __builtin_amdgcn_raw_buffer_load_b128(rg, off, 0, kAuxSc1);
    old23 = __builtin_amdgcn_raw_buffer_load_b128(rg, off, 8192, kAuxSc1);
    oldst = __builtin_amdgcn_raw_buffer_load_b64(rg, (2048 + qr * 2 + (d4 & 1)) * 8, 0, kAuxSc1);
  } else {
    r0 = __hip_atomic_load(nonce_word, RLX_AGENT);
  }
  __builtin_amdgcn_sched_barrier(0);

  const __amdgpu_buffer_rsrc_t rq = __builtin_amdgcn_make_buffer_rsrc((void*)(Q + (size_t)head * (kRows * kD * 2)), 0, kRows * kD * 2, kRsrcWord3);
  const __amdgpu_buffer_rsrc_t rk = __builtin_amdgcn_make_buffer_rsrc((void*)(K + (size_t)head * kSlab), 0, kSlab, kRsrcWord3);
  const __amdgpu_buffer_rsrc_t rv = __builtin_amdgcn_make_buffer_rsrc((void*)(V + (size_t)head * kSlab), 0, kSlab, kRsrcWord3);

  const unsigned base = ((CONSUMER ? 0u : 64u) + ((unsigned)w >> 2) * 32u) * 4096u + ((unsigned)w & 3u) * 1024u;
  auto block_off = [&](int t) -> unsigned { return base + (t < 32 ? t : 64 - kExtra + t) * 4096u; };
  const int koff = ((lane >> 2) & 3) * 256 + g * 64 + (lane & 3) * 16;
  const int voff = lane * 16;

  u32x4 qf[4];
#pragma unroll
  for (int kk = 0; kk < 4; ++kk) {
    qf[kk] = __builtin_amdgcn_raw_buffer_load_b128(rq, col * 256 + g * 64 + kk * 16, 0, 0);
    __builtin_amdgcn_sched_barrier(0);
  }

  u32x4 buf[2 * NL];
  auto issue = [&](int t) {
    if (t < NL) buf[t] = __builtin_amdgcn_raw_buffer_load_b128(rk, koff, block_off(t), kAuxNt);
    else buf[t] = __builtin_amdgcn_raw_buffer_load_b128(rv, voff, block_off(t - NL), kAuxNt);
    __builtin_amdgcn_sched_barrier(0);
  };
#pragma unroll
  for (int t = 0; t < kInFlight && t < 2 * NL; ++t) issue(t);

  unsigned nonce = 0;
  if constexpr (CONSUMER) {
    nonce = __builtin_amdgcn_readfirstlane(old01[1]);
    bool clash;
    do {
      nonce += 2 * kTagRange;
      clash = (old01[1] - nonce <= kTagRange) | (old01[3] - nonce <= kTagRange) | (old23[1] - nonce <= kTagRange) |
              (old23[3] - nonce <= kTagRange) | (oldst[1] - nonce <= kTagRange);
    } while (__any(clash));
    if (lane == 0) __hip_atomic_store(nonce_word, nonce, RLX_AGENT);
    __builtin_amdgcn_sched_barrier(0);
  }

  float s[NL];
  const f32x4 zero = {0.f, 0.f, 0.f, 0.f};
#pragma unroll
  for (int t0 = 0; t0 < NL; t0 += 4) {
#pragma unroll
    for (int t = t0; t < t0 + 4 && t < NL; ++t) {
      const f32x4 d0 = mfma32(buf[t], qf[0], zero);
      const f32x4 d1 = mfma32(buf[t], qf[1], zero);
      const f32x4 d2 = mfma32(buf[t], qf[2], zero);
      const f32x4 d3 = mfma32(buf[t], qf[3], zero);
      s[t] = (d0[0] + d1[1]) + (d2[2] + d3[3]);
      asm volatile("" : "+v"(s[t]));
    }
    __builtin_amdgcn_sched_barrier(0);
#pragma unroll
    for (int t = t0; t < t0 + 4 && t < NL; ++t)
      if (t + kInFlight < 2 * NL) issue(t + kInFlight);
    if (t0 + 4 == ((NL / 2) & ~3)) __builtin_amdgcn_s_setprio(2);
  }
  __builtin_amdgcn_s_setprio(1);
  if constexpr (CONSUMER) {
    if (lane == 0) __hip_atomic_store(nonce_word, nonce + 1, RLX_AGENT);
    __builtin_amdgcn_sched_barrier(0);
  }

  float m = s[0];
#pragma unroll
  for (int t = 1; t < NL; ++t) m = fmaxf(m, s[t]);
  m = fmaxf(m, __shfl_xor(m, 16));
  m = fmaxf(m, __shfl_xor(m, 32));
  const float mc = m * kScaleLog2e;
  float l = 0.f;
  unsigned pk[(NL + 1) / 2];
#pragma unroll
  for (int t = 0; t < NL; t += 2) {
    const float p0 = __builtin_amdgcn_exp2f(fmaf(s[t], kScaleLog2e, -mc));
    const float p1 = t + 1 < NL ? __builtin_amdgcn_exp2f(fmaf(s[t + 1 < NL ? t + 1 : t], kScaleLog2e, -mc)) : 0.f;
    l += p0 + p1;
    pk[t / 2] = pack_bf16(p0, p1);
  }
  l += __shfl_xor(l, 16);
  l += __shfl_xor(l, 32);
  if (g == 0) {
    sm.m[col * kWaves + w] = mc;
    sm.l[col * kWaves + w] = l;
  }
  __builtin_amdgcn_sched_barrier(0);

  u64 in[5];
  unsigned r1;
  auto fetch_remote = [&]() {
    if constexpr (CONSUMER) {
#pragma unroll
      for (int j = 0; j < 4; ++j) in[j] = __hip_atomic_load(my_gran + 512 * j, RLX_AGENT);
      in[4] = __hip_atomic_load(my_stat, RLX_AGENT);
    } else {
      r1 = __hip_atomic_load(nonce_word, RLX_AGENT);
    }
    __builtin_amdgcn_sched_barrier(0);
  };

  f32x4 acc[8];
#pragma unroll
  for (int dd = 0; dd < 8; ++dd) acc[dd] = zero;
#pragma unroll
  for (int u = 0; u < kSteps; ++u) {
    if (u == (kSweepAt < kSteps ? kSweepAt : kSteps - 1)) fetch_remote();
    const u32x4* vv = &buf[NL + 8 * u];
    const int n = NL - 8 * u < 8 ? NL - 8 * u : 8;
    if (n > 4) {
      const u32x4 pb = {pk[4 * u], pk[4 * u + 1], pk[4 * u + 2], n > 6 ? pk[4 * u + 3] : 0u};
#pragma unroll
      for (int dd = 0; dd < 8; ++dd) {
        const unsigned sel = (dd & 1) ? 0x07060302u : 0x05040100u;
        u32x4 a;
#pragma unroll
        for (int j = 0; j < 4; ++j) a[j] = 2 * j < n ? __builtin_amdgcn_perm(2 * j + 1 < n ? vv[2 * j + 1][dd >> 1] : 0u, vv[2 * j][dd >> 1], sel) : 0u;
        acc[dd] = mfma32(a, pb, acc[dd]);
      }
    } else {
      const u32x2 pb = {pk[4 * u], n > 2 ? pk[4 * u + 1] : 0u};
#pragma unroll
      for (int dd = 0; dd < 8; ++dd) {
        const unsigned sel = (dd & 1) ? 0x07060302u : 0x05040100u;
        u32x2 a;
#pragma unroll
        for (int j = 0; j < 2; ++j) a[j] = 2 * j < n ? __builtin_amdgcn_perm(2 * j + 1 < n ? vv[2 * j + 1][dd >> 1] : 0u, vv[2 * j][dd >> 1], sel) : 0u;
        acc[dd] = mfma16(a, pb, acc[dd]);
      }
    }
    if (u == kSteps / 2 - 1) __builtin_amdgcn_s_setprio(0);
    __builtin_amdgcn_sched_barrier(0);
#pragma unroll
    for (int t = NL + 8 * u; t < NL + 8 * u + 8; ++t)
      if (t + kInFlight < 2 * NL) issue(t + kInFlight);
  }

  float* myO = sm.o + (w * kRows + col) * kRowStride + 32 * g;
#pragma unroll
  for (int r = 0; r < 4; ++r) {
    *(f32x4*)(myO + 8 * r) = (f32x4){acc[0][r], acc[1][r], acc[2][r], acc[3][r]};
    *(f32x4*)(myO + 8 * r + 4) = (f32x4){acc[4][r], acc[5][r], acc[6][r], acc[7][r]};
  }

  if constexpr (CONSUMER) {
    unsigned bumps = 1;
    for (unsigned spins = 0; spins < kSpinCap; ++spins) {
      bool ok = true;
#pragma unroll
      for (int j = 0; j < 5; ++j) ok &= ((unsigned)(in[j] >> 32) - nonce) <= kTagRange;
      if (__all(ok)) break;
      while (spins < kSpinCap && !__all(((unsigned)(in[4] >> 32) - nonce) <= kTagRange)) {
        if ((++spins & 7) == 0) {
          ++bumps;
          if (lane == 0) __hip_atomic_store(nonce_word, nonce + bumps, RLX_AGENT);
        }
        __builtin_amdgcn_s_sleep(kPollSleep);
        in[4] = __hip_atomic_load(my_stat, RLX_AGENT);
      }
#pragma unroll
      for (int j = 0; j < 4; ++j) in[j] = __hip_atomic_load(my_gran + 512 * j, RLX_AGENT);
      in[4] = __hip_atomic_load(my_stat, RLX_AGENT);
    }
  } else {
    for (unsigned spins = 0; spins < kSpinCap * 8 && __builtin_amdgcn_readfirstlane(r1) == __builtin_amdgcn_readfirstlane(r0); ++spins) {
      __builtin_amdgcn_s_sleep(2);
      r1 = __hip_atomic_load(nonce_word, RLX_AGENT);
    }
  }
  __syncthreads();

  const f32x4 m0 = *(const f32x4*)(sm.m + qr * kWaves), m1 = *(const f32x4*)(sm.m + qr * kWaves + 4);
  const f32x4 l0 = *(const f32x4*)(sm.l + qr * kWaves), l1 = *(const f32x4*)(sm.l + qr * kWaves + 4);
  const float mw[8] = {m0[0], m0[1], m0[2], m0[3], m1[0], m1[1], m1[2], m1[3]};
  const float lw[8] = {l0[0], l0[1], l0[2], l0[3], l1[0], l1[1], l1[2], l1[3]};
  float M = mw[0];
#pragma unroll
  for (int i = 1; i < 8; ++i) M = fmaxf(M, mw[i]);
  float rm = 0.f, rl = 0.f;
  if constexpr (CONSUMER) {
    const float mine = __builtin_bit_cast(float, (unsigned)in[4]);
    const float other = __builtin_bit_cast(float, __builtin_amdgcn_update_dpp(0, (int)(unsigned)in[4], 0xB1, 0xF, 0xF, true));
    rm = (d4 & 1) ? other : mine;
    rl = (d4 & 1) ? mine : other;
    M = fmaxf(M, rm);
  }
  float L = 0.f;
  f32x4 o = zero;
#pragma unroll
  for (int i = 0; i < 8; ++i) {
    const float f = __builtin_amdgcn_exp2f(mw[i] - M);
    L = fmaf(lw[i], f, L);
    const f32x4 x = *(const f32x4*)(sm.o + (i * kRows + qr) * kRowStride + 4 * d4);
    o += x * f;
  }
  if constexpr (CONSUMER) {
    const float f = __builtin_amdgcn_exp2f(rm - M);
    L = fmaf(rl, f, L);
#pragma unroll
    for (int j = 0; j < 4; ++j) o[j] = fmaf(__builtin_bit_cast(float, (unsigned)in[j]), f, o[j]);
    const float inv = 1.0f / L;
    o *= inv;
    out[(size_t)head * (kRows * kD / 4) + tid] = (u32x2){pack_bf16(o[0], o[1]), pack_bf16(o[2], o[3])};
  } else {
    const unsigned tag = r1;
#pragma unroll
    for (int j = 0; j < 4; ++j) __hip_atomic_store(my_gran + 512 * j, granule(o[j], tag), RLX_AGENT);
    if (d4 < 2) __hip_atomic_store(my_stat, granule(d4 ? L : M, tag), RLX_AGENT);
  }
}

__global__ __launch_bounds__(kWaves * 64, 2) void gqa_decode(const char* __restrict__ Q, const char* __restrict__ K,
                                                             const char* __restrict__ V, char* __restrict__ ws,
                                                             u32x2* __restrict__ out) {
  __shared__ __attribute__((aligned(16))) Smem sm;
  const unsigned wg = blockIdx.x;
  __builtin_amdgcn_s_setprio(3);
  const unsigned head = ((wg >> 4) & 7) | ((wg & 7) << 3) | ((wg >> 7) << 6);
  const bool producer = (wg >> 3) & 1;
  if (producer) attend<kLoadsB, false>(Q, K, V, ws, out, head, sm);
  else attend<kLoadsA, true>(Q, K, V, ws, out, head, sm);
}

extern "C" void kernel_launch(void* const* d_in, const int* in_sizes, int n_in, void* d_out, int out_size, void* d_ws,
                              size_t ws_size, hipStream_t stream) {
  gqa_decode<<<2 * kHeads, kWaves * 64, 0, stream>>>((const char*)d_in[0], (const char*)d_in[1], (const char*)d_in[2],
                                                     (char*)d_ws, (u32x2*)d_out);
}
```

```cpp
#include <hip/hip_runtime.h>
#include <hip/hip_bf16.h>
#include <stdint.h>

typedef unsigned u32x4 __attribute__((ext_vector_type(4)));
typedef unsigned u32x2 __attribute__((ext_vector_type(2)));
typedef float f32x4 __attribute__((ext_vector_type(4)));
typedef float f32x2 __attribute__((ext_vector_type(2)));
typedef __bf16 bf16x8 __attribute__((ext_vector_type(8)));
typedef __bf16 bf16x2 __attribute__((ext_vector_type(2)));

constexpr int kHeads = 128;
constexpr int kD = 128;
constexpr int kRows = 16;
constexpr unsigned kSlab = 2048u * 256u;
constexpr int kWaves = 8;
constexpr int kLoads = 64;
constexpr int kWindowK = 44;
constexpr int kScoreV = 20;
constexpr int kPostV = 20;
constexpr int kWindowV = kScoreV + kPostV;
constexpr int kQueryAt = 8;
constexpr int kGroups = 2;
constexpr int kPerGroup = kLoads / kGroups;
constexpr float kScaleLog2e = 0.08838834764831845f * 1.4426950408889634f;
constexpr int kRsrcWord3 = 0x00020000;
constexpr int kAuxNt = 2;

#define WAVE_BYTES(j, h) (((((j) & 1u) << 2) | (((j) >> 1) << 7) | ((h) << 4)) * 256u)
#define LOAD_BYTES(t)                                                                                     \
  (((((t) & 1) << 3) | ((((t) >> 1) & 1) << 6) | ((((t) >> 2) & 1) << 8) | ((((t) >> 3) & 1) << 5) |      \
    ((((t) >> 4) & 1) << 9) | ((((t) >> 5) & 1) << 10)) * 256)

__device__ __forceinline__ f32x4 mfma32(u32x4 a, u32x4 b, f32x4 c) {
  return __builtin_amdgcn_mfma_f32_16x16x32_bf16(__builtin_bit_cast(bf16x8, a), __builtin_bit_cast(bf16x8, b), c, 0, 0, 0);
}
__device__ __forceinline__ unsigned pack_bf16(float lo, float hi) {
  return __builtin_bit_cast(unsigned, __builtin_convertvector((f32x2){lo, hi}, bf16x2));
}
__device__ __forceinline__ void lds_wait() { asm volatile("s_waitcnt lgkmcnt(0)" ::: "memory"); }
__device__ __forceinline__ void lds_barrier() { asm volatile("s_waitcnt lgkmcnt(0)\n\ts_barrier" ::: "memory"); }

struct Smem {
  f32x4 part[kWaves][4][64];
  u32x4 q[kRows * 16];
  float m[kRows][kWaves];
  float l[kRows][kWaves];
  unsigned arrived[2];
};

template <bool LATE>
__device__ __forceinline__ void attend(const char* __restrict__ Q, const char* __restrict__ K, const char* __restrict__ V,
                                       char* __restrict__ out, unsigned head, unsigned role, Smem& sm) {
  __builtin_amdgcn_s_setprio(LATE ? 2 : 3);

  const int tid = threadIdx.x, lane = tid & 63;
  const int w = __builtin_amdgcn_readfirstlane(tid >> 6);
  const unsigned j = (unsigned)w & 3u;
  const int g = lane >> 4, col = lane & 15;

  if (LATE) lds_barrier();
  else if (tid < 2) sm.arrived[tid] = 0;

  const __amdgpu_buffer_rsrc_t rq = __builtin_amdgcn_make_buffer_rsrc((void*)(Q + (size_t)head * (kRows * kD * 2)), 0, kRows * kD * 2, kRsrcWord3);
  const __amdgpu_buffer_rsrc_t rk = __builtin_amdgcn_make_buffer_rsrc((void*)(K + (size_t)head * kSlab), 0, kSlab, kRsrcWord3);
  const __amdgpu_buffer_rsrc_t rv = __builtin_amdgcn_make_buffer_rsrc((void*)(V + (size_t)head * kSlab), 0, kSlab, kRsrcWord3);

  const unsigned base = WAVE_BYTES(j, LATE ? 1u : 0u);
  const int koff = ((lane >> 2) & 3) * 256 + g * 64 + (lane & 3) * 16;
  const int voff = g * 256 + col * 8 + role * 128;

  u32x4 qf[4], qpiece;
  if (!LATE) {
    qpiece = __builtin_amdgcn_raw_buffer_load_b128(rq, lane * 16, j * 1024, kAuxNt);
    __builtin_amdgcn_sched_barrier(0);
  }
  u32x4 kb[kLoads];
  u32x2 vb[kLoads];
  auto issue_k = [&](int t) {
    kb[t] = __builtin_amdgcn_raw_buffer_load_b128(rk, koff, base + LOAD_BYTES(t), kAuxNt);
    __builtin_amdgcn_sched_barrier(0);
  };
  auto issue_v = [&](int t) {
    vb[t] = __builtin_amdgcn_raw_buffer_load_b64(rv, voff, base + LOAD_BYTES(t), kAuxNt);
    __builtin_amdgcn_sched_barrier(0);
  };
#pragma unroll
  for (int t = 0; t < kWindowK; ++t) {
    if (!LATE && t == kQueryAt) {
      const int r = 4 * j + g;
      sm.q[r * 16 + (col ^ r)] = qpiece;
      lds_barrier();
    }
    issue_k(t);
  }
#pragma unroll
  for (int kk = 0; kk < 4; ++kk) qf[kk] = sm.q[col * 16 + ((4 * g + kk) ^ col)];
  __builtin_amdgcn_sched_barrier(0);

  float s[kLoads], mc[kGroups], lsum[kGroups];
  unsigned pk[kLoads / 2];
  const f32x4 zero = {0.f, 0.f, 0.f, 0.f};
  auto softmax = [&](int gi) {
    float m = s[gi * kPerGroup];
#pragma unroll
    for (int t = gi * kPerGroup + 1; t < (gi + 1) * kPerGroup; ++t) m = fmaxf(m, s[t]);
    m = fmaxf(m, __shfl_xor(m, 16));
    m = fmaxf(m, __shfl_xor(m, 32));
    mc[gi] = m * kScaleLog2e;
    float l = 0.f;
#pragma unroll
    for (int t = gi * kPerGroup; t < (gi + 1) * kPerGroup; t += 2) {
      const float p0 = __builtin_amdgcn_exp2f(fmaf(s[t], kScaleLog2e, -mc[gi]));
      const float p1 = __builtin_amdgcn_exp2f(fmaf(s[t + 1], kScaleLog2e, -mc[gi]));
      l += p0 + p1;
      pk[t / 2] = pack_bf16(p0, p1);
      asm volatile("" : "+v"(pk[t / 2]));
    }
    lsum[gi] = l;
    asm volatile("" : "+v"(lsum[gi]));
    __builtin_amdgcn_sched_barrier(0);
  };
#pragma unroll
  for (int t0 = 0; t0 < kLoads; t0 += 4) {
#pragma unroll
    for (int t = t0; t < t0 + 4; ++t) {
      const f32x4 d0 = mfma32(kb[t], qf[0], zero);
      const f32x4 d1 = mfma32(kb[t], qf[1], zero);
      const f32x4 d2 = mfma32(kb[t], qf[2], zero);
      const f32x4 d3 = mfma32(kb[t], qf[3], zero);
      s[t] = (d0[0] + d1[1]) + (d2[2] + d3[3]);
      asm volatile("" : "+v"(s[t]));
    }
    __builtin_amdgcn_sched_barrier(0);
#pragma unroll
    for (int t = t0; t < t0 + 4; ++t) {
      if (t + kWindowK < kLoads) {
        issue_k(t + kWindowK);
      } else {
        const int v0 = (t + kWindowK - kLoads) * kScoreV / kWindowK, v1 = (t + 1 + kWindowK - kLoads) * kScoreV / kWindowK;
#pragma unroll
        for (int v = v0; v < v1; ++v) issue_v(v);
      }
    }
    if ((t0 + 4) % kPerGroup == 0 && t0 + 4 < kLoads) softmax((t0 + 4) / kPerGroup - 1);
  }
  __builtin_amdgcn_s_setprio(LATE ? 0 : 1);
  softmax(kGroups - 1);

  float mw = mc[0], fg[kGroups];
#pragma unroll
  for (int gi = 1; gi < kGroups; ++gi) mw = fmaxf(mw, mc[gi]);
  float l = 0.f;
#pragma unroll
  for (int gi = 0; gi < kGroups; ++gi) {
    fg[gi] = __builtin_amdgcn_exp2f(mc[gi] - mw);
    l = fmaf(lsum[gi], fg[gi], l);
  }
  l += __shfl_xor(l, 16);
  l += __shfl_xor(l, 32);
  if (g == 0) {
    sm.m[col][w] = mw;
    sm.l[col][w] = l;
  }
  if (LATE) {
    lds_wait();
    if (lane == 0) __hip_atomic_fetch_add(&sm.arrived[1], 1u, __ATOMIC_RELAXED, __HIP_MEMORY_SCOPE_WORKGROUP);
  }
  __builtin_amdgcn_sched_barrier(0);
#pragma unroll
  for (int v = kScoreV; v < kWindowV; ++v) issue_v(v);

  f32x4 accg[kGroups][4];
#pragma unroll
  for (int gi = 0; gi < kGroups; ++gi)
#pragma unroll
    for (int dd = 0; dd < 4; ++dd) accg[gi][dd] = zero;
#pragma unroll
  for (int u = 0; u < 8; ++u) {
    const u32x4 pb = (u32x4){pk[4 * u], pk[4 * u + 1], pk[4 * u + 2], pk[4 * u + 3]};
    const int gi = u * kGroups / 8;
#pragma unroll
    for (int dd = 0; dd < 4; ++dd) {
      const unsigned sel = (dd & 1) ? 0x07060302u : 0x05040100u;
      u32x4 a;
#pragma unroll
      for (int i = 0; i < 4; ++i) a[i] = __builtin_amdgcn_perm(vb[8 * u + 2 * i + 1][dd >> 1], vb[8 * u + 2 * i][dd >> 1], sel);
      accg[gi][dd] = mfma32(a, pb, accg[gi][dd]);
    }
#pragma unroll
    for (int dd = 0; dd < 4; ++dd) asm volatile("" : "+v"(accg[gi][dd]));
    __builtin_amdgcn_sched_barrier(0);
#pragma unroll
    for (int i = 8 * u + kWindowV; i < 8 * u + kWindowV + 8; ++i)
      if (i < kLoads) issue_v(i);
  }
  f32x4 acc[4];
#pragma unroll
  for (int dd = 0; dd < 4; ++dd) {
    acc[dd] = accg[0][dd] * fg[0];
#pragma unroll
    for (int gi = 1; gi < kGroups; ++gi) acc[dd] += accg[gi][dd] * fg[gi];
  }

#pragma unroll
  for (int r = 0; r < 4; ++r) sm.part[w][g][r * 16 + col] = (f32x4){acc[0][r], acc[1][r], acc[2][r], acc[3][r]};

  if constexpr (LATE) {
    lds_barrier();
  } else {
    auto wait_for = [&](int which) {
      while (__builtin_amdgcn_readfirstlane(__hip_atomic_load(&sm.arrived[which], __ATOMIC_RELAXED, __HIP_MEMORY_SCOPE_WORKGROUP)) < 4u)
        __builtin_amdgcn_s_sleep(2);
      asm volatile("" ::: "memory");
    };
    lds_wait();
    if (lane == 0) __hip_atomic_fetch_add(&sm.arrived[0], 1u, __ATOMIC_RELAXED, __HIP_MEMORY_SCOPE_WORKGROUP);
    wait_for(0);
    wait_for(1);
    const f32x4 m0 = *(const f32x4*)&sm.m[col][0], m1 = *(const f32x4*)&sm.m[col][4];
    const f32x4 l0 = *(const f32x4*)&sm.l[col][0], l1 = *(const f32x4*)&sm.l[col][4];
    const float M = fmaxf(fmaxf(fmaxf(m0[0], m0[1]), fmaxf(m0[2], m0[3])), fmaxf(fmaxf(m1[0], m1[1]), fmaxf(m1[2], m1[3])));
    float f0[4], f1[4];
    float L = 0.f;
#pragma unroll
    for (int i = 0; i < 4; ++i) {
      f0[i] = __builtin_amdgcn_exp2f(m0[i] - M);
      f1[i] = __builtin_amdgcn_exp2f(m1[i] - M);
      L = fmaf(l0[i], f0[i], L);
      L = fmaf(l1[i], f1[i], L);
    }
    const float inv = 1.0f / L;
    f32x4 o = zero;
#pragma unroll
    for (int i = 0; i < 4; ++i) {
      o += sm.part[i][j][lane] * (f0[i] * inv);
      f1[i] *= inv;
    }
    asm volatile("" : "+v"(o), "+v"(f1[0]), "+v"(f1[1]), "+v"(f1[2]), "+v"(f1[3]));
    __builtin_amdgcn_s_setprio(3);
    lds_barrier();
    o = (o + sm.part[4][j][lane] * f1[0] + sm.part[5][j][lane] * f1[1]) + (sm.part[6][j][lane] * f1[2] + sm.part[7][j][lane] * f1[3]);
    const __amdgpu_buffer_rsrc_t ro = __builtin_amdgcn_make_buffer_rsrc((void*)(out + (size_t)head * (kRows * kD * 2)), 0, kRows * kD * 2, kRsrcWord3);
    __builtin_amdgcn_raw_buffer_store_b64((u32x2){pack_bf16(o[0], o[1]), pack_bf16(o[2], o[3])}, ro,
                                          col * (kD * 2) + role * 128 + j * 32 + g * 8, 0, 0);
  }
}

__global__ __launch_bounds__(kWaves * 64, 2) void gqa_decode(const char* __restrict__ Q, const char* __restrict__ K,
                                                             const char* __restrict__ V, char* __restrict__ out) {
  __shared__ __attribute__((aligned(16))) Smem sm;
  unsigned long long t_begin;
  asm volatile("s_memrealtime %0\n s_waitcnt lgkmcnt(0)" : "=s"(t_begin) :: "memory");
  __builtin_amdgcn_sched_barrier(0);
  const unsigned wg = blockIdx.x;
  const unsigned head = ((wg >> 4) & 7) | ((wg & 7) << 3) | ((wg >> 7) << 6);
  const unsigned role = (wg >> 3) & 1;
  if (__builtin_amdgcn_readfirstlane(threadIdx.x) >= 256) attend<true>(Q, K, V, out, head, role, sm);
  else attend<false>(Q, K, V, out, head, role, sm);
  while (__builtin_amdgcn_s_memrealtime() - t_begin < 4000) __builtin_amdgcn_s_sleep(1);
}

extern "C" void kernel_launch(void* const* d_in, const int* in_sizes, int n_in, void* d_out, int out_size, void* d_ws,
                              size_t ws_size, hipStream_t stream) {
  gqa_decode<<<2 * kHeads, kWaves * 64, 0, stream>>>((const char*)d_in[0], (const char*)d_in[1], (const char*)d_in[2],
                                                     (char*)d_out);
}
```

```cpp
#include <hip/hip_runtime.h>
#include <hip/hip_bf16.h>
#include <stdint.h>

typedef unsigned u32x4 __attribute__((ext_vector_type(4)));
typedef unsigned u32x2 __attribute__((ext_vector_type(2)));
typedef float f32x4 __attribute__((ext_vector_type(4)));
typedef float f32x2 __attribute__((ext_vector_type(2)));
typedef __bf16 bf16x8 __attribute__((ext_vector_type(8)));
typedef __bf16 bf16x2 __attribute__((ext_vector_type(2)));

constexpr int kHeads = 128;
constexpr int kD = 128;
constexpr int kRows = 16;
constexpr unsigned kSlab = 2048u * 256u;
constexpr int kWaves = 8;
constexpr int kLoads = 64;
constexpr int kWindowK = 44;
constexpr int kScoreV = 20;
constexpr int kPostV = 20;
constexpr int kWindowV = kScoreV + kPostV;
constexpr int kQueryAt = 8;
constexpr int kGroups = 2;
constexpr int kPerGroup = kLoads / kGroups;
constexpr float kScaleLog2e = 0.08838834764831845f * 1.4426950408889634f;
constexpr int kRsrcWord3 = 0x00020000;
constexpr int kAuxNt = 2;

#define WAVE_BYTES(j, h) (((((j) & 1u) << 2) | (((j) >> 1) << 7) | ((h) << 4)) * 256u)
#define LOAD_BYTES(t)                                                                                     \
  (((((t) & 1) << 3) | ((((t) >> 1) & 1) << 6) | ((((t) >> 2) & 1) << 8) | ((((t) >> 3) & 1) << 5) |      \
    ((((t) >> 4) & 1) << 9) | ((((t) >> 5) & 1) << 10)) * 256)

__device__ __forceinline__ f32x4 mfma32(u32x4 a, u32x4 b, f32x4 c) {
  return __builtin_amdgcn_mfma_f32_16x16x32_bf16(__builtin_bit_cast(bf16x8, a), __builtin_bit_cast(bf16x8, b), c, 0, 0, 0);
}
__device__ __forceinline__ unsigned pack_bf16(float lo, float hi) {
  return __builtin_bit_cast(unsigned, __builtin_convertvector((f32x2){lo, hi}, bf16x2));
}
__device__ __forceinline__ void lds_wait() { asm volatile("s_waitcnt lgkmcnt(0)" ::: "memory"); }
__device__ __forceinline__ void lds_barrier() { asm volatile("s_waitcnt lgkmcnt(0)\n\ts_barrier" ::: "memory"); }

struct Smem {
  f32x4 part[kWaves][4][64];
  u32x4 q[kRows * 16];
  float m[kRows][kWaves];
  float l[kRows][kWaves];
  unsigned arrived[2];
};

template <bool LATE>
__device__ __forceinline__ void attend(const char* __restrict__ Q, const char* __restrict__ K, const char* __restrict__ V,
                                       char* __restrict__ out, unsigned head, unsigned role, Smem& sm) {
  __builtin_amdgcn_s_setprio(LATE ? 2 : 3);

  const int tid = threadIdx.x, lane = tid & 63;
  const int w = __builtin_amdgcn_readfirstlane(tid >> 6);
  const unsigned j = (unsigned)w & 3u;
  const int g = lane >> 4, col = lane & 15;

  if (LATE) lds_barrier();
  else if (tid < 2) sm.arrived[tid] = 0;

  const __amdgpu_buffer_rsrc_t rq = __builtin_amdgcn_make_buffer_rsrc((void*)(Q + (size_t)head * (kRows * kD * 2)), 0, kRows * kD * 2, kRsrcWord3);
  const __amdgpu_buffer_rsrc_t rk = __builtin_amdgcn_make_buffer_rsrc((void*)(K + (size_t)head * kSlab), 0, kSlab, kRsrcWord3);
  const __amdgpu_buffer_rsrc_t rv = __builtin_amdgcn_make_buffer_rsrc((void*)(V + (size_t)head * kSlab), 0, kSlab, kRsrcWord3);

  const unsigned base = WAVE_BYTES(j, LATE ? 1u : 0u);
  const int koff = ((lane >> 2) & 3) * 256 + g * 64 + (lane & 3) * 16;
  const int voff = g * 256 + col * 8 + role * 128;

  u32x4 qf[4], qpiece;
  if (!LATE) {
    qpiece = __builtin_amdgcn_raw_buffer_load_b128(rq, lane * 16, j * 1024, kAuxNt);
    __builtin_amdgcn_sched_barrier(0);
  }
  u32x4 kb[kLoads];
  u32x2 vb[kLoads];
  auto issue_k = [&](int t) {
    kb[t] = __builtin_amdgcn_raw_buffer_load_b128(rk, koff, base + LOAD_BYTES(t), kAuxNt);
    __builtin_amdgcn_sched_barrier(0);
  };
  auto issue_v = [&](int t) {
    vb[t] = __builtin_amdgcn_raw_buffer_load_b64(rv, voff, base + LOAD_BYTES(t), kAuxNt);
    __builtin_amdgcn_sched_barrier(0);
  };
#pragma unroll
  for (int t = 0; t < kWindowK; ++t) {
    if (!LATE && t == kQueryAt) {
      const int r = 4 * j + g;
      sm.q[r * 16 + (col ^ r)] = qpiece;
      lds_barrier();
    }
    issue_k(t);
  }
#pragma unroll
  for (int kk = 0; kk < 4; ++kk) qf[kk] = sm.q[col * 16 + ((4 * g + kk) ^ col)];
  __builtin_amdgcn_sched_barrier(0);

  float s[kLoads], mc[kGroups], lsum[kGroups];
  unsigned pk[kLoads / 2];
  const f32x4 zero = {0.f, 0.f, 0.f, 0.f};
  auto softmax = [&](int gi) {
    float m = s[gi * kPerGroup];
#pragma unroll
    for (int t = gi * kPerGroup + 1; t < (gi + 1) * kPerGroup; ++t) m = fmaxf(m, s[t]);
    m = fmaxf(m, __shfl_xor(m, 16));
    m = fmaxf(m, __shfl_xor(m, 32));
    mc[gi] = m * kScaleLog2e;
    float l = 0.f;
#pragma unroll
    for (int t = gi * kPerGroup; t < (gi + 1) * kPerGroup; t += 2) {
      const float p0 = __builtin_amdgcn_exp2f(fmaf(s[t], kScaleLog2e, -mc[gi]));
      const float p1 = __builtin_amdgcn_exp2f(fmaf(s[t + 1], kScaleLog2e, -mc[gi]));
      l += p0 + p1;
      pk[t / 2] = pack_bf16(p0, p1);
      asm volatile("" : "+v"(pk[t / 2]));
    }
    lsum[gi] = l;
    asm volatile("" : "+v"(lsum[gi]));
    __builtin_amdgcn_sched_barrier(0);
  };
#pragma unroll
  for (int t0 = 0; t0 < kLoads; t0 += 4) {
#pragma unroll
    for (int t = t0; t < t0 + 4; ++t) {
      const f32x4 d0 = mfma32(kb[t], qf[0], zero);
      const f32x4 d1 = mfma32(kb[t], qf[1], zero);
      const f32x4 d2 = mfma32(kb[t], qf[2], zero);
      const f32x4 d3 = mfma32(kb[t], qf[3], zero);
      s[t] = (d0[0] + d1[1]) + (d2[2] + d3[3]);
      asm volatile("" : "+v"(s[t]));
    }
    __builtin_amdgcn_sched_barrier(0);
#pragma unroll
    for (int t = t0; t < t0 + 4; ++t) {
      if (t + kWindowK < kLoads) {
        issue_k(t + kWindowK);
      } else {
        const int v0 = (t + kWindowK - kLoads) * kScoreV / kWindowK, v1 = (t + 1 + kWindowK - kLoads) * kScoreV / kWindowK;
#pragma unroll
        for (int v = v0; v < v1; ++v) issue_v(v);
      }
    }
    if ((t0 + 4) % kPerGroup == 0 && t0 + 4 < kLoads) softmax((t0 + 4) / kPerGroup - 1);
  }
  __builtin_amdgcn_s_setprio(LATE ? 0 : 1);
  softmax(kGroups - 1);

  float mw = mc[0], fg[kGroups];
#pragma unroll
  for (int gi = 1; gi < kGroups; ++gi) mw = fmaxf(mw, mc[gi]);
  float l = 0.f;
#pragma unroll
  for (int gi = 0; gi < kGroups; ++gi) {
    fg[gi] = __builtin_amdgcn_exp2f(mc[gi] - mw);
    l = fmaf(lsum[gi], fg[gi], l);
  }
  l += __shfl_xor(l, 16);
  l += __shfl_xor(l, 32);
  if (g == 0) {
    sm.m[col][w] = mw;
    sm.l[col][w] = l;
  }
  if (LATE) {
    lds_wait();
    if (lane == 0) __hip_atomic_fetch_add(&sm.arrived[1], 1u, __ATOMIC_RELAXED, __HIP_MEMORY_SCOPE_WORKGROUP);
  }
  __builtin_amdgcn_sched_barrier(0);
#pragma unroll
  for (int v = kScoreV; v < kWindowV; ++v) issue_v(v);

  f32x4 accg[kGroups][4];
#pragma unroll
  for (int gi = 0; gi < kGroups; ++gi)
#pragma unroll
    for (int dd = 0; dd < 4; ++dd) accg[gi][dd] = zero;
#pragma unroll
  for (int u = 0; u < 8; ++u) {
    const u32x4 pb = (u32x4){pk[4 * u], pk[4 * u + 1], pk[4 * u + 2], pk[4 * u + 3]};
    const int gi = u * kGroups / 8;
#pragma unroll
    for (int dd = 0; dd < 4; ++dd) {
      const unsigned sel = (dd & 1) ? 0x07060302u : 0x05040100u;
      u32x4 a;
#pragma unroll
      for (int i = 0; i < 4; ++i) a[i] = __builtin_amdgcn_perm(vb[8 * u + 2 * i + 1][dd >> 1], vb[8 * u + 2 * i][dd >> 1], sel);
      accg[gi][dd] = mfma32(a, pb, accg[gi][dd]);
    }
#pragma unroll
    for (int dd = 0; dd < 4; ++dd) asm volatile("" : "+v"(accg[gi][dd]));
    __builtin_amdgcn_sched_barrier(0);
#pragma unroll
    for (int i = 8 * u + kWindowV; i < 8 * u + kWindowV + 8; ++i)
      if (i < kLoads) issue_v(i);
  }
  f32x4 acc[4];
#pragma unroll
  for (int dd = 0; dd < 4; ++dd) {
    acc[dd] = accg[0][dd] * fg[0];
#pragma unroll
    for (int gi = 1; gi < kGroups; ++gi) acc[dd] += accg[gi][dd] * fg[gi];
  }

#pragma unroll
  for (int r = 0; r < 4; ++r) sm.part[w][g][r * 16 + col] = (f32x4){acc[0][r], acc[1][r], acc[2][r], acc[3][r]};

  if constexpr (LATE) {
    lds_barrier();
  } else {
    auto wait_for = [&](int which) {
      while (__builtin_amdgcn_readfirstlane(__hip_atomic_load(&sm.arrived[which], __ATOMIC_RELAXED, __HIP_MEMORY_SCOPE_WORKGROUP)) < 4u)
        __builtin_amdgcn_s_sleep(2);
      asm volatile("" ::: "memory");
    };
    lds_wait();
    if (lane == 0) __hip_atomic_fetch_add(&sm.arrived[0], 1u, __ATOMIC_RELAXED, __HIP_MEMORY_SCOPE_WORKGROUP);
    wait_for(0);
    wait_for(1);
    const f32x4 m0 = *(const f32x4*)&sm.m[col][0], m1 = *(const f32x4*)&sm.m[col][4];
    const f32x4 l0 = *(const f32x4*)&sm.l[col][0], l1 = *(const f32x4*)&sm.l[col][4];
    const float M = fmaxf(fmaxf(fmaxf(m0[0], m0[1]), fmaxf(m0[2], m0[3])), fmaxf(fmaxf(m1[0], m1[1]), fmaxf(m1[2], m1[3])));
    float f0[4], f1[4];
    float L = 0.f;
#pragma unroll
    for (int i = 0; i < 4; ++i) {
      f0[i] = __builtin_amdgcn_exp2f(m0[i] - M);
      f1[i] = __builtin_amdgcn_exp2f(m1[i] - M);
      L = fmaf(l0[i], f0[i], L);
      L = fmaf(l1[i], f1[i], L);
    }
    const float inv = 1.0f / L;
    f32x4 o = zero;
#pragma unroll
    for (int i = 0; i < 4; ++i) {
      o += sm.part[i][j][lane] * (f0[i] * inv);
      f1[i] *= inv;
    }
    asm volatile("" : "+v"(o), "+v"(f1[0]), "+v"(f1[1]), "+v"(f1[2]), "+v"(f1[3]));
    __builtin_amdgcn_s_setprio(3);
    lds_barrier();
    o = (o + sm.part[4][j][lane] * f1[0] + sm.part[5][j][lane] * f1[1]) + (sm.part[6][j][lane] * f1[2] + sm.part[7][j][lane] * f1[3]);
    const __amdgpu_buffer_rsrc_t ro = __builtin_amdgcn_make_buffer_rsrc((void*)(out + (size_t)head * (kRows * kD * 2)), 0, kRows * kD * 2, kRsrcWord3);
    __builtin_amdgcn_raw_buffer_store_b64((u32x2){pack_bf16(o[0], o[1]), pack_bf16(o[2], o[3])}, ro,
                                          col * (kD * 2) + role * 128 + j * 32 + g * 8, 0, 0);
  }
}

__global__ __launch_bounds__(kWaves * 64, 2) void gqa_decode(const char* __restrict__ Q, const char* __restrict__ K,
                                                             const char* __restrict__ V, char* __restrict__ ws,
                                                             char* __restrict__ out) {
  __shared__ __attribute__((aligned(16))) Smem sm;
  const unsigned wg = blockIdx.x;
  const unsigned head = ((wg >> 4) & 7) | ((wg & 7) << 3) | ((wg >> 7) << 6);
  const unsigned role = (wg >> 3) & 1;
  if (__builtin_amdgcn_readfirstlane(threadIdx.x) >= 256) attend<true>(Q, K, V, out, head, role, sm);
  else attend<false>(Q, K, V, out, head, role, sm);
}

extern "C" void kernel_launch(void* const* d_in, const int* in_sizes, int n_in, void* d_out, int out_size, void* d_ws,
                              size_t ws_size, hipStream_t stream) {
  gqa_decode<<<2 * kHeads, kWaves * 64, 0, stream>>>((const char*)d_in[0], (const char*)d_in[1], (const char*)d_in[2],
                                                     (char*)d_ws, (char*)d_out);
}
```

```cpp
#include <hip/hip_runtime.h>
#include <hip/hip_bf16.h>
#include <stdint.h>

typedef unsigned u32x4 __attribute__((ext_vector_type(4)));
typedef unsigned u32x2 __attribute__((ext_vector_type(2)));
typedef float f32x4 __attribute__((ext_vector_type(4)));
typedef float f32x2 __attribute__((ext_vector_type(2)));
typedef __bf16 bf16x8 __attribute__((ext_vector_type(8)));
typedef __bf16 bf16x2 __attribute__((ext_vector_type(2)));

constexpr int kHeads = 128;
constexpr int kD = 128;
constexpr int kRows = 16;
constexpr unsigned kSlab = 2048u * 256u;
constexpr int kWaves = 8;
constexpr int kLoads = 64;
constexpr int kWindowK = 44;
constexpr int kScoreV = 20;
constexpr int kPostV = 20;
constexpr int kWindowV = kScoreV + kPostV;
constexpr int kQueryAt = 8;
constexpr int kGroups = 2;
constexpr int kPerGroup = kLoads / kGroups;
constexpr float kScaleLog2e = 0.08838834764831845f * 1.4426950408889634f;
constexpr int kRsrcWord3 = 0x00020000;
constexpr int kAuxNt = 2;

#define WAVE_BYTES(j, h) (((((j) & 1u) << 2) | (((j) >> 1) << 7) | ((h) << 4)) * 256u)
#define LOAD_BYTES(t)                                                                                     \
  (((((t) & 1) << 3) | ((((t) >> 1) & 1) << 6) | ((((t) >> 2) & 1) << 8) | ((((t) >> 3) & 1) << 5) |      \
    ((((t) >> 4) & 1) << 9) | ((((t) >> 5) & 1) << 10)) * 256)

__device__ __forceinline__ f32x4 mfma32(u32x4 a, u32x4 b, f32x4 c) {
  return __builtin_amdgcn_mfma_f32_16x16x32_bf16(__builtin_bit_cast(bf16x8, a), __builtin_bit_cast(bf16x8, b), c, 0, 0, 0);
}
__device__ __forceinline__ unsigned pack_bf16(float lo, float hi) {
  return __builtin_bit_cast(unsigned, __builtin_convertvector((f32x2){lo, hi}, bf16x2));
}
__device__ __forceinline__ void lds_wait() { asm volatile("s_waitcnt lgkmcnt(0)" ::: "memory"); }
__device__ __forceinline__ void lds_barrier() { asm volatile("s_waitcnt lgkmcnt(0)\n\ts_barrier" ::: "memory"); }

struct Smem {
  f32x4 part[kWaves][4][64];
  u32x4 q[kRows * 16];
  float m[kRows][kWaves];
  float l[kRows][kWaves];
  unsigned arrived[2];
};

template <bool LATE>
__device__ __forceinline__ void attend(const char* __restrict__ Q, const char* __restrict__ K, const char* __restrict__ V,
                                       char* __restrict__ out, unsigned head, unsigned role, Smem& sm) {
  __builtin_amdgcn_s_setprio(LATE ? 2 : 3);

  const int tid = threadIdx.x, lane = tid & 63;
  const int w = __builtin_amdgcn_readfirstlane(tid >> 6);
  const unsigned j = (unsigned)w & 3u;
  const int g = lane >> 4, col = lane & 15;

  if (LATE) lds_barrier();
  else if (tid < 2) sm.arrived[tid] = 0;

  const __amdgpu_buffer_rsrc_t rq = __builtin_amdgcn_make_buffer_rsrc((void*)(Q + (size_t)head * (kRows * kD * 2)), 0, kRows * kD * 2, kRsrcWord3);
  const __amdgpu_buffer_rsrc_t rk = __builtin_amdgcn_make_buffer_rsrc((void*)(K + (size_t)head * kSlab), 0, kSlab, kRsrcWord3);
  const __amdgpu_buffer_rsrc_t rv = __builtin_amdgcn_make_buffer_rsrc((void*)(V + (size_t)head * kSlab), 0, kSlab, kRsrcWord3);

  const unsigned base = WAVE_BYTES(j, LATE ? 1u : 0u);
  const int koff = ((lane >> 2) & 3) * 256 + g * 64 + (lane & 3) * 16;
  const int voff = g * 256 + col * 8 + role * 128;

  u32x4 qf[4], qpiece;
  if (!LATE) {
    qpiece = __builtin_amdgcn_raw_buffer_load_b128(rq, lane * 16, j * 1024, kAuxNt);
    __builtin_amdgcn_sched_barrier(0);
  }
  u32x4 kb[kLoads];
  u32x2 vb[kLoads];
  auto issue_k = [&](int t) {
    kb[t] = __builtin_amdgcn_raw_buffer_load_b128(rk, koff, base + LOAD_BYTES(t), kAuxNt);
    __builtin_amdgcn_sched_barrier(0);
  };
  auto issue_v = [&](int t) {
    vb[t] = __builtin_amdgcn_raw_buffer_load_b64(rv, voff, base + LOAD_BYTES(t), kAuxNt);
    __builtin_amdgcn_sched_barrier(0);
  };
#pragma unroll
  for (int t = 0; t < kWindowK; ++t) {
    if (!LATE && t == kQueryAt) {
      const int r = 4 * j + g;
      sm.q[r * 16 + (col ^ r)] = qpiece;
      lds_barrier();
    }
    issue_k(t);
  }
#pragma unroll
  for (int kk = 0; kk < 4; ++kk) qf[kk] = sm.q[col * 16 + ((4 * g + kk) ^ col)];
  __builtin_amdgcn_sched_barrier(0);

  float s[kLoads], mc[kGroups], lsum[kGroups];
  unsigned pk[kLoads / 2];
  const f32x4 zero = {0.f, 0.f, 0.f, 0.f};
  auto softmax = [&](int gi) {
    float m = s[gi * kPerGroup];
#pragma unroll
    for (int t = gi * kPerGroup + 1; t < (gi + 1) * kPerGroup; ++t) m = fmaxf(m, s[t]);
    m = fmaxf(m, __shfl_xor(m, 16));
    m = fmaxf(m, __shfl_xor(m, 32));
    mc[gi] = m * kScaleLog2e;
    float l = 0.f;
#pragma unroll
    for (int t = gi * kPerGroup; t < (gi + 1) * kPerGroup; t += 2) {
      const float p0 = __builtin_amdgcn_exp2f(fmaf(s[t], kScaleLog2e, -mc[gi]));
      const float p1 = __builtin_amdgcn_exp2f(fmaf(s[t + 1], kScaleLog2e, -mc[gi]));
      l += p0 + p1;
      pk[t / 2] = pack_bf16(p0, p1);
      asm volatile("" : "+v"(pk[t / 2]));
    }
    lsum[gi] = l;
    asm volatile("" : "+v"(lsum[gi]));
    __builtin_amdgcn_sched_barrier(0);
  };
#pragma unroll
  for (int t0 = 0; t0 < kLoads; t0 += 4) {
#pragma unroll
    for (int t = t0; t < t0 + 4; ++t) {
      const f32x4 d0 = mfma32(kb[t], qf[0], zero);
      const f32x4 d1 = mfma32(kb[t], qf[1], zero);
      const f32x4 d2 = mfma32(kb[t], qf[2], zero);
      const f32x4 d3 = mfma32(kb[t], qf[3], zero);
      s[t] = (d0[0] + d1[1]) + (d2[2] + d3[3]);
      asm volatile("" : "+v"(s[t]));
    }
    __builtin_amdgcn_sched_barrier(0);
#pragma unroll
    for (int t = t0; t < t0 + 4; ++t) {
      if (t + kWindowK < kLoads) {
        issue_k(t + kWindowK);
      } else {
        const int v0 = (t + kWindowK - kLoads) * kScoreV / kWindowK, v1 = (t + 1 + kWindowK - kLoads) * kScoreV / kWindowK;
#pragma unroll
        for (int v = v0; v < v1; ++v) issue_v(v);
      }
    }
    if ((t0 + 4) % kPerGroup == 0 && t0 + 4 < kLoads) softmax((t0 + 4) / kPerGroup - 1);
  }
  __builtin_amdgcn_s_setprio(LATE ? 0 : 1);
  softmax(kGroups - 1);

  float mw = mc[0], fg[kGroups];
#pragma unroll
  for (int gi = 1; gi < kGroups; ++gi) mw = fmaxf(mw, mc[gi]);
  float l = 0.f;
#pragma unroll
  for (int gi = 0; gi < kGroups; ++gi) {
    fg[gi] = __builtin_amdgcn_exp2f(mc[gi] - mw);
    l = fmaf(lsum[gi], fg[gi], l);
  }
  l += __shfl_xor(l, 16);
  l += __shfl_xor(l, 32);
  if (g == 0) {
    sm.m[col][w] = mw;
    sm.l[col][w] = l;
  }
  if (LATE) {
    lds_wait();
    if (lane == 0) __hip_atomic_fetch_add(&sm.arrived[1], 1u, __ATOMIC_RELAXED, __HIP_MEMORY_SCOPE_WORKGROUP);
  }
  __builtin_amdgcn_sched_barrier(0);
#pragma unroll
  for (int v = kScoreV; v < kWindowV; ++v) issue_v(v);

  f32x4 accg[kGroups][4];
#pragma unroll
  for (int gi = 0; gi < kGroups; ++gi)
#pragma unroll
    for (int dd = 0; dd < 4; ++dd) accg[gi][dd] = zero;
#pragma unroll
  for (int u = 0; u < 8; ++u) {
    const u32x4 pb = (u32x4){pk[4 * u], pk[4 * u + 1], pk[4 * u + 2], pk[4 * u + 3]};
    const int gi = u * kGroups / 8;
#pragma unroll
    for (int dd = 0; dd < 4; ++dd) {
      const unsigned sel = (dd & 1) ? 0x07060302u : 0x05040100u;
      u32x4 a;
#pragma unroll
      for (int i = 0; i < 4; ++i) a[i] = __builtin_amdgcn_perm(vb[8 * u + 2 * i + 1][dd >> 1], vb[8 * u + 2 * i][dd >> 1], sel);
      accg[gi][dd] = mfma32(a, pb, accg[gi][dd]);
    }
#pragma unroll
    for (int dd = 0; dd < 4; ++dd) asm volatile("" : "+v"(accg[gi][dd]));
    __builtin_amdgcn_sched_barrier(0);
#pragma unroll
    for (int i = 8 * u + kWindowV; i < 8 * u + kWindowV + 8; ++i)
      if (i < kLoads) issue_v(i);
  }
  f32x4 acc[4];
#pragma unroll
  for (int dd = 0; dd < 4; ++dd) {
    acc[dd] = accg[0][dd] * fg[0];
#pragma unroll
    for (int gi = 1; gi < kGroups; ++gi) acc[dd] += accg[gi][dd] * fg[gi];
  }

#pragma unroll
  for (int r = 0; r < 4; ++r) sm.part[w][g][r * 16 + col] = (f32x4){acc[0][r], acc[1][r], acc[2][r], acc[3][r]};

  if constexpr (LATE) {
    lds_barrier();
  } else {
    auto wait_for = [&](int which) {
      while (__builtin_amdgcn_readfirstlane(__hip_atomic_load(&sm.arrived[which], __ATOMIC_RELAXED, __HIP_MEMORY_SCOPE_WORKGROUP)) < 4u)
        __builtin_amdgcn_s_sleep(2);
      asm volatile("" ::: "memory");
    };
    lds_wait();
    if (lane == 0) __hip_atomic_fetch_add(&sm.arrived[0], 1u, __ATOMIC_RELAXED, __HIP_MEMORY_SCOPE_WORKGROUP);
    wait_for(0);
    wait_for(1);
    const f32x4 m0 = *(const f32x4*)&sm.m[col][0], m1 = *(const f32x4*)&sm.m[col][4];
    const f32x4 l0 = *(const f32x4*)&sm.l[col][0], l1 = *(const f32x4*)&sm.l[col][4];
    const float M = fmaxf(fmaxf(fmaxf(m0[0], m0[1]), fmaxf(m0[2], m0[3])), fmaxf(fmaxf(m1[0], m1[1]), fmaxf(m1[2], m1[3])));
    float f0[4], f1[4];
    float L = 0.f;
#pragma unroll
    for (int i = 0; i < 4; ++i) {
      f0[i] = __builtin_amdgcn_exp2f(m0[i] - M);
      f1[i] = __builtin_amdgcn_exp2f(m1[i] - M);
      L = fmaf(l0[i], f0[i], L);
      L = fmaf(l1[i], f1[i], L);
    }
    const float inv = 1.0f / L;
    f32x4 o = zero;
#pragma unroll
    for (int i = 0; i < 4; ++i) {
      o += sm.part[i][j][lane] * (f0[i] * inv);
      f1[i] *= inv;
    }
    asm volatile("" : "+v"(o), "+v"(f1[0]), "+v"(f1[1]), "+v"(f1[2]), "+v"(f1[3]));
    __builtin_amdgcn_s_setprio(3);
    lds_barrier();
    o = (o + sm.part[4][j][lane] * f1[0] + sm.part[5][j][lane] * f1[1]) + (sm.part[6][j][lane] * f1[2] + sm.part[7][j][lane] * f1[3]);
    const __amdgpu_buffer_rsrc_t ro = __builtin_amdgcn_make_buffer_rsrc((void*)(out + (size_t)head * (kRows * kD * 2)), 0, kRows * kD * 2, kRsrcWord3);
    __builtin_amdgcn_raw_buffer_store_b64((u32x2){pack_bf16(o[0], o[1]), pack_bf16(o[2], o[3])}, ro,
                                          col * (kD * 2) + role * 128 + j * 32 + g * 8, 0, 0);
  }
}

typedef unsigned u32x8 __attribute__((ext_vector_type(8)));
__global__ __launch_bounds__(kWaves * 64, 2) void gqa_decode(const char* __restrict__, const char* __restrict__,
                                                             const char* __restrict__, char* __restrict__) {
  __shared__ __attribute__((aligned(16))) Smem sm;
  unsigned long long t_begin;
  u32x8 a;
  asm volatile("s_memrealtime %0\n\ts_waitcnt lgkmcnt(0)\n\ts_load_dwordx8 %1, %2, 0x0\n\ts_waitcnt lgkmcnt(0)"
               : "=&s"(t_begin), "=&s"(a) : "s"(__builtin_amdgcn_kernarg_segment_ptr()) : "memory");
  const unsigned a0 = a[0], a1 = a[1], a2 = a[2], a3 = a[3], a4 = a[4], a5 = a[5], a6 = a[6], a7 = a[7];
  const char* __restrict__ Q = (const char*)(((uint64_t)a1 << 32) | a0);
  const char* __restrict__ K = (const char*)(((uint64_t)a3 << 32) | a2);
  const char* __restrict__ V = (const char*)(((uint64_t)a5 << 32) | a4);
  char* __restrict__ out = (char*)(((uint64_t)a7 << 32) | a6);
  const unsigned wg = blockIdx.x;
  const unsigned head = ((wg >> 4) & 7) | ((wg & 7) << 3) | ((wg >> 7) << 6);
  const unsigned role = (wg >> 3) & 1;
  if (__builtin_amdgcn_readfirstlane(threadIdx.x) >= 256) attend<true>(Q, K, V, out, head, role, sm);
  else attend<false>(Q, K, V, out, head, role, sm);
  while (__builtin_amdgcn_s_memrealtime() - t_begin < 4000) __builtin_amdgcn_s_sleep(1);
}

extern "C" void kernel_launch(void* const* d_in, const int* in_sizes, int n_in, void* d_out, int out_size, void* d_ws,
                              size_t ws_size, hipStream_t stream) {
  gqa_decode<<<2 * kHeads, kWaves * 64, 0, stream>>>((const char*)d_in[0], (const char*)d_in[1], (const char*)d_in[2],
                                                     (char*)d_out);
}
```

```cpp
#include <hip/hip_runtime.h>
#include <hip/hip_bf16.h>
#include <stdint.h>

typedef unsigned u32x4 __attribute__((ext_vector_type(4)));
typedef unsigned u32x2 __attribute__((ext_vector_type(2)));
typedef float f32x4 __attribute__((ext_vector_type(4)));
typedef float f32x2 __attribute__((ext_vector_type(2)));
typedef __bf16 bf16x8 __attribute__((ext_vector_type(8)));
typedef __bf16 bf16x2 __attribute__((ext_vector_type(2)));

constexpr int kHeads = 128;
constexpr int kD = 128;
constexpr int kRows = 16;
constexpr unsigned kSlab = 2048u * 256u;
constexpr int kWaves = 8;
constexpr int kLoads = 64;
constexpr int kWindowK = 44;
constexpr int kScoreV = 20;
constexpr int kPostV = 20;
constexpr int kWindowV = kScoreV + kPostV;
constexpr int kQueryAt = 8;
constexpr int kGroups = 2;
constexpr int kPerGroup = kLoads / kGroups;
constexpr float kScaleLog2e = 0.08838834764831845f * 1.4426950408889634f;
constexpr int kRsrcWord3 = 0x00020000;
constexpr int kAuxNt = 2;

#define WAVE_BYTES(j, h) (((((j) & 1u) << 2) | (((j) >> 1) << 7) | ((h) << 4)) * 256u)
#define LOAD_BYTES(t)                                                                                     \
  (((((t) & 1) << 3) | ((((t) >> 1) & 1) << 6) | ((((t) >> 2) & 1) << 8) | ((((t) >> 3) & 1) << 5) |      \
    ((((t) >> 4) & 1) << 9) | ((((t) >> 5) & 1) << 10)) * 256)

__device__ __forceinline__ f32x4 mfma32(u32x4 a, u32x4 b, f32x4 c) {
  return __builtin_amdgcn_mfma_f32_16x16x32_bf16(__builtin_bit_cast(bf16x8, a), __builtin_bit_cast(bf16x8, b), c, 0, 0, 0);
}
__device__ __forceinline__ unsigned pack_bf16(float lo, float hi) {
  return __builtin_bit_cast(unsigned, __builtin_convertvector((f32x2){lo, hi}, bf16x2));
}
__device__ __forceinline__ void lds_wait() { asm volatile("s_waitcnt lgkmcnt(0)" ::: "memory"); }
__device__ __forceinline__ void lds_barrier() { asm volatile("s_waitcnt lgkmcnt(0)\n\ts_barrier" ::: "memory"); }

struct Smem {
  f32x4 part[kWaves][4][64];
  u32x4 q[kRows * 16];
  float m[kRows][kWaves];
  float l[kRows][kWaves];
  unsigned arrived[2];
};

template <bool LATE>
__device__ __forceinline__ void attend(const char* __restrict__ Q, const char* __restrict__ K, const char* __restrict__ V,
                                       char* __restrict__ out, unsigned head, unsigned role, Smem& sm) {
  __builtin_amdgcn_s_setprio(LATE ? 2 : 3);

  const int tid = threadIdx.x, lane = tid & 63;
  const int w = __builtin_amdgcn_readfirstlane(tid >> 6);
  const unsigned j = (unsigned)w & 3u;
  const int g = lane >> 4, col = lane & 15;

  if (LATE) lds_barrier();
  else if (tid < 2) sm.arrived[tid] = 0;

  const __amdgpu_buffer_rsrc_t rq = __builtin_amdgcn_make_buffer_rsrc((void*)(Q + (size_t)head * (kRows * kD * 2)), 0, kRows * kD * 2, kRsrcWord3);
  const __amdgpu_buffer_rsrc_t rk = __builtin_amdgcn_make_buffer_rsrc((void*)(K + (size_t)head * kSlab), 0, kSlab, kRsrcWord3);
  const __amdgpu_buffer_rsrc_t rv = __builtin_amdgcn_make_buffer_rsrc((void*)(V + (size_t)head * kSlab), 0, kSlab, kRsrcWord3);

  const unsigned base = WAVE_BYTES(j, LATE ? 1u : 0u);
  const int koff = ((lane >> 2) & 3) * 256 + g * 64 + (lane & 3) * 16;
  const int voff = g * 256 + col * 8 + role * 128;

  u32x4 qf[4], qpiece;
  if (!LATE) {
    qpiece = __builtin_amdgcn_raw_buffer_load_b128(rq, lane * 16, j * 1024, kAuxNt);
    __builtin_amdgcn_sched_barrier(0);
  }
  u32x4 kb[kLoads];
  u32x2 vb[kLoads];
  auto issue_k = [&](int t) {
    kb[t] = __builtin_amdgcn_raw_buffer_load_b128(rk, koff, base + LOAD_BYTES(t), kAuxNt);
    __builtin_amdgcn_sched_barrier(0);
  };
  auto issue_v = [&](int t) {
    vb[t] = __builtin_amdgcn_raw_buffer_load_b64(rv, voff, base + LOAD_BYTES(t), kAuxNt);
    __builtin_amdgcn_sched_barrier(0);
  };
#pragma unroll
  for (int t = 0; t < kWindowK; ++t) {
    if (!LATE && t == kQueryAt) {
      const int r = 4 * j + g;
      sm.q[r * 16 + (col ^ r)] = qpiece;
      lds_barrier();
    }
    issue_k(t);
  }
#pragma unroll
  for (int kk = 0; kk < 4; ++kk) qf[kk] = sm.q[col * 16 + ((4 * g + kk) ^ col)];
  __builtin_amdgcn_sched_barrier(0);

  float s[kLoads], mc[kGroups], lsum[kGroups];
  unsigned pk[kLoads / 2];
  const f32x4 zero = {0.f, 0.f, 0.f, 0.f};
  auto softmax = [&](int gi) {
    float m = s[gi * kPerGroup];
#pragma unroll
    for (int t = gi * kPerGroup + 1; t < (gi + 1) * kPerGroup; ++t) m = fmaxf(m, s[t]);
    m = fmaxf(m, __shfl_xor(m, 16));
    m = fmaxf(m, __shfl_xor(m, 32));
    mc[gi] = m * kScaleLog2e;
    float l = 0.f;
#pragma unroll
    for (int t = gi * kPerGroup; t < (gi + 1) * kPerGroup; t += 2) {
      const float p0 = __builtin_amdgcn_exp2f(fmaf(s[t], kScaleLog2e, -mc[gi]));
      const float p1 = __builtin_amdgcn_exp2f(fmaf(s[t + 1], kScaleLog2e, -mc[gi]));
      l += p0 + p1;
      pk[t / 2] = pack_bf16(p0, p1);
      asm volatile("" : "+v"(pk[t / 2]));
    }
    lsum[gi] = l;
    asm volatile("" : "+v"(lsum[gi]));
    __builtin_amdgcn_sched_barrier(0);
  };
#pragma unroll
  for (int t0 = 0; t0 < kLoads; t0 += 4) {
#pragma unroll
    for (int t = t0; t < t0 + 4; ++t) {
      const f32x4 d0 = mfma32(kb[t], qf[0], zero);
      const f32x4 d1 = mfma32(kb[t], qf[1], zero);
      const f32x4 d2 = mfma32(kb[t], qf[2], zero);
      const f32x4 d3 = mfma32(kb[t], qf[3], zero);
      s[t] = (d0[0] + d1[1]) + (d2[2] + d3[3]);
      asm volatile("" : "+v"(s[t]));
    }
    __builtin_amdgcn_sched_barrier(0);
#pragma unroll
    for (int t = t0; t < t0 + 4; ++t) {
      if (t + kWindowK < kLoads) {
        issue_k(t + kWindowK);
      } else {
        const int v0 = (t + kWindowK - kLoads) * kScoreV / kWindowK, v1 = (t + 1 + kWindowK - kLoads) * kScoreV / kWindowK;
#pragma unroll
        for (int v = v0; v < v1; ++v) issue_v(v);
      }
    }
    if ((t0 + 4) % kPerGroup == 0 && t0 + 4 < kLoads) softmax((t0 + 4) / kPerGroup - 1);
  }
  __builtin_amdgcn_s_setprio(LATE ? 0 : 1);
  softmax(kGroups - 1);

  float mw = mc[0], fg[kGroups];
#pragma unroll
  for (int gi = 1; gi < kGroups; ++gi) mw = fmaxf(mw, mc[gi]);
  float l = 0.f;
#pragma unroll
  for (int gi = 0; gi < kGroups; ++gi) {
    fg[gi] = __builtin_amdgcn_exp2f(mc[gi] - mw);
    l = fmaf(lsum[gi], fg[gi], l);
  }
  l += __shfl_xor(l, 16);
  l += __shfl_xor(l, 32);
  if (g == 0) {
    sm.m[col][w] = mw;
    sm.l[col][w] = l;
  }
  if (LATE) {
    lds_wait();
    if (lane == 0) __hip_atomic_fetch_add(&sm.arrived[1], 1u, __ATOMIC_RELAXED, __HIP_MEMORY_SCOPE_WORKGROUP);
  }
  __builtin_amdgcn_sched_barrier(0);
#pragma unroll
  for (int v = kScoreV; v < kWindowV; ++v) issue_v(v);

  f32x4 accg[kGroups][4];
#pragma unroll
  for (int gi = 0; gi < kGroups; ++gi)
#pragma unroll
    for (int dd = 0; dd < 4; ++dd) accg[gi][dd] = zero;
#pragma unroll
  for (int u = 0; u < 8; ++u) {
    const u32x4 pb = (u32x4){pk[4 * u], pk[4 * u + 1], pk[4 * u + 2], pk[4 * u + 3]};
    const int gi = u * kGroups / 8;
#pragma unroll
    for (int dd = 0; dd < 4; ++dd) {
      const unsigned sel = (dd & 1) ? 0x07060302u : 0x05040100u;
      u32x4 a;
#pragma unroll
      for (int i = 0; i < 4; ++i) a[i] = __builtin_amdgcn_perm(vb[8 * u + 2 * i + 1][dd >> 1], vb[8 * u + 2 * i][dd >> 1], sel);
      accg[gi][dd] = mfma32(a, pb, accg[gi][dd]);
    }
#pragma unroll
    for (int dd = 0; dd < 4; ++dd) asm volatile("" : "+v"(accg[gi][dd]));
    __builtin_amdgcn_sched_barrier(0);
#pragma unroll
    for (int i = 8 * u + kWindowV; i < 8 * u + kWindowV + 8; ++i)
      if (i < kLoads) issue_v(i);
  }
  f32x4 acc[4];
#pragma unroll
  for (int dd = 0; dd < 4; ++dd) {
    acc[dd] = accg[0][dd] * fg[0];
#pragma unroll
    for (int gi = 1; gi < kGroups; ++gi) acc[dd] += accg[gi][dd] * fg[gi];
  }

#pragma unroll
  for (int r = 0; r < 4; ++r) sm.part[w][g][r * 16 + col] = (f32x4){acc[0][r], acc[1][r], acc[2][r], acc[3][r]};

  if constexpr (LATE) {
    lds_barrier();
  } else {
    auto wait_for = [&](int which) {
      while (__builtin_amdgcn_readfirstlane(__hip_atomic_load(&sm.arrived[which], __ATOMIC_RELAXED, __HIP_MEMORY_SCOPE_WORKGROUP)) < 4u)
        __builtin_amdgcn_s_sleep(2);
      asm volatile("" ::: "memory");
    };
    lds_wait();
    if (lane == 0) __hip_atomic_fetch_add(&sm.arrived[0], 1u, __ATOMIC_RELAXED, __HIP_MEMORY_SCOPE_WORKGROUP);
    wait_for(0);
    wait_for(1);
    const f32x4 m0 = *(const f32x4*)&sm.m[col][0], m1 = *(const f32x4*)&sm.m[col][4];
    const f32x4 l0 = *(const f32x4*)&sm.l[col][0], l1 = *(const f32x4*)&sm.l[col][4];
    const float M = fmaxf(fmaxf(fmaxf(m0[0], m0[1]), fmaxf(m0[2], m0[3])), fmaxf(fmaxf(m1[0], m1[1]), fmaxf(m1[2], m1[3])));
    float f0[4], f1[4];
    float L = 0.f;
#pragma unroll
    for (int i = 0; i < 4; ++i) {
      f0[i] = __builtin_amdgcn_exp2f(m0[i] - M);
      f1[i] = __builtin_amdgcn_exp2f(m1[i] - M);
      L = fmaf(l0[i], f0[i], L);
      L = fmaf(l1[i], f1[i], L);
    }
    const float inv = 1.0f / L;
    f32x4 o = zero;
#pragma unroll
    for (int i = 0; i < 4; ++i) {
      o += sm.part[i][j][lane] * (f0[i] * inv);
      f1[i] *= inv;
    }
    asm volatile("" : "+v"(o), "+v"(f1[0]), "+v"(f1[1]), "+v"(f1[2]), "+v"(f1[3]));
    __builtin_amdgcn_s_setprio(3);
    lds_barrier();
    o = (o + sm.part[4][j][lane] * f1[0] + sm.part[5][j][lane] * f1[1]) + (sm.part[6][j][lane] * f1[2] + sm.part[7][j][lane] * f1[3]);
    const __amdgpu_buffer_rsrc_t ro = __builtin_amdgcn_make_buffer_rsrc((void*)(out + (size_t)head * (kRows * kD * 2)), 0, kRows * kD * 2, kRsrcWord3);
    __builtin_amdgcn_raw_buffer_store_b64((u32x2){pack_bf16(o[0], o[1]), pack_bf16(o[2], o[3])}, ro,
                                          col * (kD * 2) + role * 128 + j * 32 + g * 8, 0, 0);
  }
}

__global__ __launch_bounds__(kWaves * 64, 2) void gqa_decode(const char* __restrict__ Q, const char* __restrict__ K,
                                                             const char* __restrict__ V, char* __restrict__ out) {
  __shared__ __attribute__((aligned(16))) Smem sm;
  const unsigned wg = blockIdx.x;
  const unsigned head = ((wg >> 4) & 7) | ((wg & 7) << 3) | ((wg >> 7) << 6);
  const unsigned role = (wg >> 3) & 1;
  if (__builtin_amdgcn_readfirstlane(threadIdx.x) >= 256) attend<true>(Q, K, V, out, head, role, sm);
  else attend<false>(Q, K, V, out, head, role, sm);
}

extern "C" void kernel_launch(void* const* d_in, const int* in_sizes, int n_in, void* d_out, int out_size, void* d_ws,
                              size_t ws_size, hipStream_t stream) {
  gqa_decode<<<2 * kHeads, kWaves * 64, 0, stream>>>((const char*)d_in[0], (const char*)d_in[1], (const char*)d_in[2],
                                                     (char*)d_out);
}
```
